```python
import torch

def _softmax(x, axis=-1):
    return torch.softmax(x, dim=axis)
def _gelu(x):
    return torch.nn.functional.gelu(x, approximate="tanh")
def _silu(x):
    return torch.nn.functional.silu(x)
def _sigmoid(x):
    return torch.sigmoid(x)
def _layernorm(x, axis=-1, eps=1e-5):
    m = x.mean(dim=axis, keepdim=True)
    v = x.var(dim=axis, keepdim=True, unbiased=False)
    return (x - m) / torch.sqrt(v + eps)
def _scatter_add(tgt, idx, src, axis=0):
    return tgt.clone().index_add_(axis, idx, src)
def _segment_sum(src, seg, n):
    out = torch.zeros((n,) + src.shape[1:], dtype=src.dtype)
    return out.index_add_(0, seg, src)
def _pad(x, pw):
    flat = tuple(v for pair in reversed(pw) for v in pair)
    return torch.nn.functional.pad(x, flat)

kernel_name = 'standalone__rmsnorm__sampling'

def setup_inputs():

    _w = (lambda a: (lambda t: t.to(torch.bfloat16) if t.is_floating_point() else t)(torch.as_tensor(a)))
    B, S, D = 1024, 16, 576
    return {
        "x": _w((lambda *s: torch.randn(*s, dtype=torch.bfloat16) * 0.05)(B, S, D)),
        "g": _w((lambda *s: torch.randn(*s, dtype=torch.bfloat16) * 0.05)(D) + 1.0),
    }

def reference(x, g):
    rms = torch.sqrt((x * x).mean(axis=-1, keepdims=True) + 1e-6)
    return x / rms * g
```

```cpp
#include <hip/hip_runtime.h>
#include <hip/hip_bf16.h>
#include <stdint.h>

typedef __attribute__((ext_vector_type(4))) unsigned int u32x4;
typedef __attribute__((ext_vector_type(2))) float f32x2;
typedef __attribute__((ext_vector_type(2))) __bf16 bf16x2;

constexpr int D = 576;
constexpr int CPR = D / 8;
constexpr int NCH = CPR / 8;

__device__ __forceinline__ float lo_f(unsigned v) { return __uint_as_float(v << 16); }
__device__ __forceinline__ float hi_f(unsigned v) { return __uint_as_float(v & 0xffff0000u); }
__device__ __forceinline__ unsigned pack_bf16(float a, float b) {
  f32x2 f = {a, b};
  bf16x2 r = __builtin_convertvector(f, bf16x2);
  return __builtin_bit_cast(unsigned, r);
}
template <int CTRL>
__device__ __forceinline__ float dpp_add(float v) {
  int r = __builtin_amdgcn_update_dpp(0, __float_as_int(v), CTRL, 0xf, 0xf, true);
  return v + __int_as_float(r);
}

__global__ __launch_bounds__(256) void rmsnorm_kernel(const u32x4* __restrict__ x,
                                                      const u32x4* __restrict__ g,
                                                      u32x4* __restrict__ out, int rows) {
  int tid = blockIdx.x * 256 + threadIdx.x;
  int row = tid >> 3;
  int sub = tid & 7;
  if (row >= rows) return;
  const u32x4* xr = x + (size_t)row * CPR + sub;
  u32x4* orow = out + (size_t)row * CPR + sub;
  u32x4 v[NCH], gv[NCH];
#pragma unroll
  for (int j = 0; j < NCH; ++j) v[j] = xr[j * 8];
#pragma unroll
  for (int j = 0; j < NCH; ++j) gv[j] = g[j * 8 + sub];
  float ss = 0.f;
#pragma unroll
  for (int j = 0; j < NCH; ++j) {
#pragma unroll
    for (int k = 0; k < 4; ++k) {
      float a = lo_f(v[j][k]), b = hi_f(v[j][k]);
      ss = fmaf(a, a, ss);
      ss = fmaf(b, b, ss);
    }
  }
  ss = dpp_add<0xB1>(ss);
  ss = dpp_add<0x4E>(ss);
  ss = dpp_add<0x141>(ss);
  float r = rsqrtf(ss * (1.0f / D) + 1e-6f);
#pragma unroll
  for (int j = 0; j < NCH; ++j) {
    u32x4 o;
#pragma unroll
    for (int k = 0; k < 4; ++k) {
      float a = lo_f(v[j][k]) * r * lo_f(gv[j][k]);
      float b = hi_f(v[j][k]) * r * hi_f(gv[j][k]);
      o[k] = pack_bf16(a, b);
    }
    orow[j * 8] = o;
  }
}

extern "C" void kernel_launch(void* const* d_in, const int* in_sizes, int n_in, void* d_out,
                              int out_size, void* d_ws, size_t ws_size, hipStream_t stream) {
  const u32x4* x = (const u32x4*)d_in[0];
  const u32x4* g = (const u32x4*)d_in[1];
  u32x4* out = (u32x4*)d_out;
  int rows = in_sizes[0] / D;
  int threads = rows * 8;
  rmsnorm_kernel<<<(threads + 255) / 256, 256, 0, stream>>>(x, g, out, rows);
}
```

```cpp
#include <hip/hip_runtime.h>
#include <stdint.h>

__global__ __launch_bounds__(256) void rmsnorm_kernel(const void* x, unsigned x_bytes, unsigned x_fmt,
                                                      const void* g, unsigned g_bytes, unsigned g_fmt,
                                                      void* out, unsigned o_bytes, unsigned o_fmt,
                                                      unsigned pad0, unsigned pad1, unsigned pad2, unsigned pad3) {
  __shared__ __attribute__((aligned(16))) char lds[4096];
  const unsigned lds_base = (unsigned)(uintptr_t)(__attribute__((address_space(3))) char*)lds;
  asm volatile(
      "s_load_dwordx16 s[4:19], %0, 0x0\n"
      "s_mul_i32 s20, %1, 36864\n"
      "v_and_b32 v2, 7, %2\n"
      "v_lshrrev_b32 v1, 3, %2\n"
      "v_mul_u32_u24 v1, 0x480, v1\n"
      "v_lshl_add_u32 v1, v2, 4, v1\n"
      "v_add_u32 v1, s20, v1\n"
      "v_lshlrev_b32 v3, 4, %2\n"
      "s_waitcnt lgkmcnt(0)\n"
      "buffer_load_dwordx4 v[8:11], v3, s[8:11], 0 offen\n"
      "buffer_load_dwordx4 v[16:19], v1, s[4:7], 0 offen offset:0 nt\n"
      "buffer_load_dwordx4 v[20:23], v1, s[4:7], 0 offen offset:128 nt\n"
      "buffer_load_dwordx4 v[24:27], v1, s[4:7], 0 offen offset:256 nt\n"
      "buffer_load_dwordx4 v[28:31], v1, s[4:7], 0 offen offset:384 nt\n"
      "buffer_load_dwordx4 v[32:35], v1, s[4:7], 0 offen offset:512 nt\n"
      "buffer_load_dwordx4 v[36:39], v1, s[4:7], 0 offen offset:640 nt\n"
      "buffer_load_dwordx4 v[40:43], v1, s[4:7], 0 offen offset:768 nt\n"
      "buffer_load_dwordx4 v[44:47], v1, s[4:7], 0 offen offset:896 nt\n"
      "buffer_load_dwordx4 v[48:51], v1, s[4:7], 0 offen offset:1024 nt\n"
      "v_mov_b32 v5, 0x3ae38e39\n"
      "v_mov_b32 v4, 0\n"
      "v_lshlrev_b32 v2, 5, v2\n"
      "v_add_u32 v2, %3, v2\n"
      "v_lshlrev_b32 v3, 1, v3\n"
      "v_add_u32 v3, %3, v3\n"
      "v_readfirstlane_b32 s21, %2\n"
      "s_cmp_ge_u32 s21, 128\n"
      "s_cbranch_scc1 L_bar%=\n"
      "s_waitcnt vmcnt(9)\n"
      "v_lshlrev_b32 v52, 16, v8\n"
      "v_and_b32 v53, 0xffff0000, v8\n"
      "v_lshlrev_b32 v54, 16, v9\n"
      "v_and_b32 v55, 0xffff0000, v9\n"
      "v_lshlrev_b32 v56, 16, v10\n"
      "v_and_b32 v57, 0xffff0000, v10\n"
      "v_lshlrev_b32 v58, 16, v11\n"
      "v_and_b32 v59, 0xffff0000, v11\n"
      "ds_write_b128 v3, v[52:55]\n"
      "ds_write_b128 v3, v[56:59] offset:16\n"
      "s_waitcnt lgkmcnt(0)\n"
      "L_bar%=:\n"
      "s_barrier\n"
      "ds_read_b128 v[52:55], v2 offset:0\n"
      "ds_read_b128 v[56:59], v2 offset:16\n"
      "ds_read_b128 v[60:63], v2 offset:256\n"
      "ds_read_b128 v[64:67], v2 offset:272\n"
      "ds_read_b128 v[68:71], v2 offset:512\n"
      "ds_read_b128 v[72:75], v2 offset:528\n"
      "ds_read_b128 v[76:79], v2 offset:768\n"
      "ds_read_b128 v[80:83], v2 offset:784\n"
      "ds_read_b128 v[84:87], v2 offset:1024\n"
      "ds_read_b128 v[88:91], v2 offset:1040\n"
      "ds_read_b128 v[92:95], v2 offset:1280\n"
      "ds_read_b128 v[96:99], v2 offset:1296\n"
      "ds_read_b128 v[100:103], v2 offset:1536\n"
      "ds_read_b128 v[104:107], v2 offset:1552\n"
      "s_waitcnt lgkmcnt(8)\n"
      "ds_read_b128 v[108:111], v2 offset:1792\n"
      "ds_read_b128 v[112:115], v2 offset:1808\n"
      "ds_read_b128 v[116:119], v2 offset:2048\n"
      "ds_read_b128 v[120:123], v2 offset:2064\n"
      "s_waitcnt lgkmcnt(0)\n"
      "s_waitcnt vmcnt(8)\n"
      "v_dot2c_f32_bf16 v4, v16, v16\n"
      "v_lshlrev_b32 v8, 16, v16\n"
      "v_and_b32 v9, 0xffff0000, v16\n"
      "v_pk_mul_f32 v[52:53], v[8:9], v[52:53]\n"
      "v_dot2c_f32_bf16 v4, v17, v17\n"
      "v_lshlrev_b32 v10, 16, v17\n"
      "v_and_b32 v11, 0xffff0000, v17\n"
      "v_pk_mul_f32 v[54:55], v[10:11], v[54:55]\n"
      "v_dot2c_f32_bf16 v4, v18, v18\n"
      "v_lshlrev_b32 v12, 16, v18\n"
      "v_and_b32 v13, 0xffff0000, v18\n"
      "v_pk_mul_f32 v[56:57], v[12:13], v[56:57]\n"
      "v_dot2c_f32_bf16 v4, v19, v19\n"
      "v_lshlrev_b32 v14, 16, v19\n"
      "v_and_b32 v15, 0xffff0000, v19\n"
      "v_pk_mul_f32 v[58:59], v[14:15], v[58:59]\n"
      "s_waitcnt vmcnt(7)\n"
      "v_dot2c_f32_bf16 v4, v20, v20\n"
      "v_lshlrev_b32 v8, 16, v20\n"
      "v_and_b32 v9, 0xffff0000, v20\n"
      "v_pk_mul_f32 v[60:61], v[8:9], v[60:61]\n"
      "v_dot2c_f32_bf16 v4, v21, v21\n"
      "v_lshlrev_b32 v10, 16, v21\n"
      "v_and_b32 v11, 0xffff0000, v21\n"
      "v_pk_mul_f32 v[62:63], v[10:11], v[62:63]\n"
      "v_dot2c_f32_bf16 v4, v22, v22\n"
      "v_lshlrev_b32 v12, 16, v22\n"
      "v_and_b32 v13, 0xffff0000, v22\n"
      "v_pk_mul_f32 v[64:65], v[12:13], v[64:65]\n"
      "v_dot2c_f32_bf16 v4, v23, v23\n"
      "v_lshlrev_b32 v14, 16, v23\n"
      "v_and_b32 v15, 0xffff0000, v23\n"
      "v_pk_mul_f32 v[66:67], v[14:15], v[66:67]\n"
      "s_waitcnt vmcnt(6)\n"
      "v_dot2c_f32_bf16 v4, v24, v24\n"
      "v_lshlrev_b32 v8, 16, v24\n"
      "v_and_b32 v9, 0xffff0000, v24\n"
      "v_pk_mul_f32 v[68:69], v[8:9], v[68:69]\n"
      "v_dot2c_f32_bf16 v4, v25, v25\n"
      "v_lshlrev_b32 v10, 16, v25\n"
      "v_and_b32 v11, 0xffff0000, v25\n"
      "v_pk_mul_f32 v[70:71], v[10:11], v[70:71]\n"
      "v_dot2c_f32_bf16 v4, v26, v26\n"
      "v_lshlrev_b32 v12, 16, v26\n"
      "v_and_b32 v13, 0xffff0000, v26\n"
      "v_pk_mul_f32 v[72:73], v[12:13], v[72:73]\n"
      "v_dot2c_f32_bf16 v4, v27, v27\n"
      "v_lshlrev_b32 v14, 16, v27\n"
      "v_and_b32 v15, 0xffff0000, v27\n"
      "v_pk_mul_f32 v[74:75], v[14:15], v[74:75]\n"
      "s_waitcnt vmcnt(5)\n"
      "v_dot2c_f32_bf16 v4, v28, v28\n"
      "v_lshlrev_b32 v8, 16, v28\n"
      "v_and_b32 v9, 0xffff0000, v28\n"
      "v_pk_mul_f32 v[76:77], v[8:9], v[76:77]\n"
      "v_dot2c_f32_bf16 v4, v29, v29\n"
      "v_lshlrev_b32 v10, 16, v29\n"
      "v_and_b32 v11, 0xffff0000, v29\n"
      "v_pk_mul_f32 v[78:79], v[10:11], v[78:79]\n"
      "v_dot2c_f32_bf16 v4, v30, v30\n"
      "v_lshlrev_b32 v12, 16, v30\n"
      "v_and_b32 v13, 0xffff0000, v30\n"
      "v_pk_mul_f32 v[80:81], v[12:13], v[80:81]\n"
      "v_dot2c_f32_bf16 v4, v31, v31\n"
      "v_lshlrev_b32 v14, 16, v31\n"
      "v_and_b32 v15, 0xffff0000, v31\n"
      "v_pk_mul_f32 v[82:83], v[14:15], v[82:83]\n"
      "s_waitcnt vmcnt(4)\n"
      "v_dot2c_f32_bf16 v4, v32, v32\n"
      "v_lshlrev_b32 v8, 16, v32\n"
      "v_and_b32 v9, 0xffff0000, v32\n"
      "v_pk_mul_f32 v[84:85], v[8:9], v[84:85]\n"
      "v_dot2c_f32_bf16 v4, v33, v33\n"
      "v_lshlrev_b32 v10, 16, v33\n"
      "v_and_b32 v11, 0xffff0000, v33\n"
      "v_pk_mul_f32 v[86:87], v[10:11], v[86:87]\n"
      "v_dot2c_f32_bf16 v4, v34, v34\n"
      "v_lshlrev_b32 v12, 16, v34\n"
      "v_and_b32 v13, 0xffff0000, v34\n"
      "v_pk_mul_f32 v[88:89], v[12:13], v[88:89]\n"
      "v_dot2c_f32_bf16 v4, v35, v35\n"
      "v_lshlrev_b32 v14, 16, v35\n"
      "v_and_b32 v15, 0xffff0000, v35\n"
      "v_pk_mul_f32 v[90:91], v[14:15], v[90:91]\n"
      "s_waitcnt vmcnt(3)\n"
      "v_dot2c_f32_bf16 v4, v36, v36\n"
      "v_lshlrev_b32 v8, 16, v36\n"
      "v_and_b32 v9, 0xffff0000, v36\n"
      "v_pk_mul_f32 v[92:93], v[8:9], v[92:93]\n"
      "v_dot2c_f32_bf16 v4, v37, v37\n"
      "v_lshlrev_b32 v10, 16, v37\n"
      "v_and_b32 v11, 0xffff0000, v37\n"
      "v_pk_mul_f32 v[94:95], v[10:11], v[94:95]\n"
      "v_dot2c_f32_bf16 v4, v38, v38\n"
      "v_lshlrev_b32 v12, 16, v38\n"
      "v_and_b32 v13, 0xffff0000, v38\n"
      "v_pk_mul_f32 v[96:97], v[12:13], v[96:97]\n"
      "v_dot2c_f32_bf16 v4, v39, v39\n"
      "v_lshlrev_b32 v14, 16, v39\n"
      "v_and_b32 v15, 0xffff0000, v39\n"
      "v_pk_mul_f32 v[98:99], v[14:15], v[98:99]\n"
      "s_waitcnt vmcnt(2)\n"
      "v_dot2c_f32_bf16 v4, v40, v40\n"
      "v_lshlrev_b32 v8, 16, v40\n"
      "v_and_b32 v9, 0xffff0000, v40\n"
      "v_pk_mul_f32 v[100:101], v[8:9], v[100:101]\n"
      "v_dot2c_f32_bf16 v4, v41, v41\n"
      "v_lshlrev_b32 v10, 16, v41\n"
      "v_and_b32 v11, 0xffff0000, v41\n"
      "v_pk_mul_f32 v[102:103], v[10:11], v[102:103]\n"
      "v_dot2c_f32_bf16 v4, v42, v42\n"
      "v_lshlrev_b32 v12, 16, v42\n"
      "v_and_b32 v13, 0xffff0000, v42\n"
      "v_pk_mul_f32 v[104:105], v[12:13], v[104:105]\n"
      "v_dot2c_f32_bf16 v4, v43, v43\n"
      "v_lshlrev_b32 v14, 16, v43\n"
      "v_and_b32 v15, 0xffff0000, v43\n"
      "v_pk_mul_f32 v[106:107], v[14:15], v[106:107]\n"
      "s_waitcnt vmcnt(1)\n"
      "v_dot2c_f32_bf16 v4, v44, v44\n"
      "v_lshlrev_b32 v8, 16, v44\n"
      "v_and_b32 v9, 0xffff0000, v44\n"
      "v_pk_mul_f32 v[108:109], v[8:9], v[108:109]\n"
      "v_dot2c_f32_bf16 v4, v45, v45\n"
      "v_lshlrev_b32 v10, 16, v45\n"
      "v_and_b32 v11, 0xffff0000, v45\n"
      "v_pk_mul_f32 v[110:111], v[10:11], v[110:111]\n"
      "v_dot2c_f32_bf16 v4, v46, v46\n"
      "v_lshlrev_b32 v12, 16, v46\n"
      "v_and_b32 v13, 0xffff0000, v46\n"
      "v_pk_mul_f32 v[112:113], v[12:13], v[112:113]\n"
      "v_dot2c_f32_bf16 v4, v47, v47\n"
      "v_lshlrev_b32 v14, 16, v47\n"
      "v_and_b32 v15, 0xffff0000, v47\n"
      "v_pk_mul_f32 v[114:115], v[14:15], v[114:115]\n"
      "s_waitcnt vmcnt(0)\n"
      "v_dot2c_f32_bf16 v4, v48, v48\n"
      "v_lshlrev_b32 v8, 16, v48\n"
      "v_and_b32 v9, 0xffff0000, v48\n"
      "v_pk_mul_f32 v[116:117], v[8:9], v[116:117]\n"
      "v_dot2c_f32_bf16 v4, v49, v49\n"
      "v_lshlrev_b32 v10, 16, v49\n"
      "v_and_b32 v11, 0xffff0000, v49\n"
      "v_pk_mul_f32 v[118:119], v[10:11], v[118:119]\n"
      "v_dot2c_f32_bf16 v4, v50, v50\n"
      "v_lshlrev_b32 v12, 16, v50\n"
      "v_and_b32 v13, 0xffff0000, v50\n"
      "v_pk_mul_f32 v[120:121], v[12:13], v[120:121]\n"
      "v_dot2c_f32_bf16 v4, v51, v51\n"
      "v_lshlrev_b32 v14, 16, v51\n"
      "v_and_b32 v15, 0xffff0000, v51\n"
      "v_pk_mul_f32 v[122:123], v[14:15], v[122:123]\n"
      "s_nop 2\n"
      "v_add_f32_dpp v4, v4, v4 quad_perm:[1,0,3,2] row_mask:0xf bank_mask:0xf\n"
      "s_nop 1\n"
      "v_add_f32_dpp v4, v4, v4 quad_perm:[2,3,0,1] row_mask:0xf bank_mask:0xf\n"
      "s_nop 1\n"
      "v_add_f32_dpp v4, v4, v4 row_half_mirror row_mask:0xf bank_mask:0xf\n"
      "v_fmaak_f32 v4, v4, v5, 0x358637bd\n"
      "v_rsq_f32 v6, v4\n"
      "s_nop 0\n"
      "v_pk_mul_f32 v[52:53], v[6:7], v[52:53] op_sel_hi:[0,1]\n"
      "v_pk_mul_f32 v[54:55], v[6:7], v[54:55] op_sel_hi:[0,1]\n"
      "v_pk_mul_f32 v[56:57], v[6:7], v[56:57] op_sel_hi:[0,1]\n"
      "v_pk_mul_f32 v[58:59], v[6:7], v[58:59] op_sel_hi:[0,1]\n"
      "v_cvt_pk_bf16_f32 v16, v52, v53\n"
      "v_cvt_pk_bf16_f32 v17, v54, v55\n"
      "v_cvt_pk_bf16_f32 v18, v56, v57\n"
      "v_cvt_pk_bf16_f32 v19, v58, v59\n"
      "buffer_store_dwordx4 v[16:19], v1, s[12:15], 0 offen offset:0 sc0 sc1 nt\n"
      "v_pk_mul_f32 v[60:61], v[6:7], v[60:61] op_sel_hi:[0,1]\n"
      "v_pk_mul_f32 v[62:63], v[6:7], v[62:63] op_sel_hi:[0,1]\n"
      "v_pk_mul_f32 v[64:65], v[6:7], v[64:65] op_sel_hi:[0,1]\n"
      "v_pk_mul_f32 v[66:67], v[6:7], v[66:67] op_sel_hi:[0,1]\n"
      "v_cvt_pk_bf16_f32 v20, v60, v61\n"
      "v_cvt_pk_bf16_f32 v21, v62, v63\n"
      "v_cvt_pk_bf16_f32 v22, v64, v65\n"
      "v_cvt_pk_bf16_f32 v23, v66, v67\n"
      "buffer_store_dwordx4 v[20:23], v1, s[12:15], 0 offen offset:128 sc0 sc1 nt\n"
      "v_pk_mul_f32 v[68:69], v[6:7], v[68:69] op_sel_hi:[0,1]\n"
      "v_pk_mul_f32 v[70:71], v[6:7], v[70:71] op_sel_hi:[0,1]\n"
      "v_pk_mul_f32 v[72:73], v[6:7], v[72:73] op_sel_hi:[0,1]\n"
      "v_pk_mul_f32 v[74:75], v[6:7], v[74:75] op_sel_hi:[0,1]\n"
      "v_cvt_pk_bf16_f32 v24, v68, v69\n"
      "v_cvt_pk_bf16_f32 v25, v70, v71\n"
      "v_cvt_pk_bf16_f32 v26, v72, v73\n"
      "v_cvt_pk_bf16_f32 v27, v74, v75\n"
      "buffer_store_dwordx4 v[24:27], v1, s[12:15], 0 offen offset:256 sc0 sc1 nt\n"
      "v_pk_mul_f32 v[76:77], v[6:7], v[76:77] op_sel_hi:[0,1]\n"
      "v_pk_mul_f32 v[78:79], v[6:7], v[78:79] op_sel_hi:[0,1]\n"
      "v_pk_mul_f32 v[80:81], v[6:7], v[80:81] op_sel_hi:[0,1]\n"
      "v_pk_mul_f32 v[82:83], v[6:7], v[82:83] op_sel_hi:[0,1]\n"
      "v_cvt_pk_bf16_f32 v28, v76, v77\n"
      "v_cvt_pk_bf16_f32 v29, v78, v79\n"
      "v_cvt_pk_bf16_f32 v30, v80, v81\n"
      "v_cvt_pk_bf16_f32 v31, v82, v83\n"
      "buffer_store_dwordx4 v[28:31], v1, s[12:15], 0 offen offset:384 sc0 sc1 nt\n"
      "v_pk_mul_f32 v[84:85], v[6:7], v[84:85] op_sel_hi:[0,1]\n"
      "v_pk_mul_f32 v[86:87], v[6:7], v[86:87] op_sel_hi:[0,1]\n"
      "v_pk_mul_f32 v[88:89], v[6:7], v[88:89] op_sel_hi:[0,1]\n"
      "v_pk_mul_f32 v[90:91], v[6:7], v[90:91] op_sel_hi:[0,1]\n"
      "v_cvt_pk_bf16_f32 v32, v84, v85\n"
      "v_cvt_pk_bf16_f32 v33, v86, v87\n"
      "v_cvt_pk_bf16_f32 v34, v88, v89\n"
      "v_cvt_pk_bf16_f32 v35, v90, v91\n"
      "buffer_store_dwordx4 v[32:35], v1, s[12:15], 0 offen offset:512 sc0 sc1 nt\n"
      "v_pk_mul_f32 v[92:93], v[6:7], v[92:93] op_sel_hi:[0,1]\n"
      "v_pk_mul_f32 v[94:95], v[6:7], v[94:95] op_sel_hi:[0,1]\n"
      "v_pk_mul_f32 v[96:97], v[6:7], v[96:97] op_sel_hi:[0,1]\n"
      "v_pk_mul_f32 v[98:99], v[6:7], v[98:99] op_sel_hi:[0,1]\n"
      "v_cvt_pk_bf16_f32 v36, v92, v93\n"
      "v_cvt_pk_bf16_f32 v37, v94, v95\n"
      "v_cvt_pk_bf16_f32 v38, v96, v97\n"
      "v_cvt_pk_bf16_f32 v39, v98, v99\n"
      "buffer_store_dwordx4 v[36:39], v1, s[12:15], 0 offen offset:640 sc0 sc1 nt\n"
      "v_pk_mul_f32 v[100:101], v[6:7], v[100:101] op_sel_hi:[0,1]\n"
      "v_pk_mul_f32 v[102:103], v[6:7], v[102:103] op_sel_hi:[0,1]\n"
      "v_pk_mul_f32 v[104:105], v[6:7], v[104:105] op_sel_hi:[0,1]\n"
      "v_pk_mul_f32 v[106:107], v[6:7], v[106:107] op_sel_hi:[0,1]\n"
      "v_cvt_pk_bf16_f32 v40, v100, v101\n"
      "v_cvt_pk_bf16_f32 v41, v102, v103\n"
      "v_cvt_pk_bf16_f32 v42, v104, v105\n"
      "v_cvt_pk_bf16_f32 v43, v106, v107\n"
      "buffer_store_dwordx4 v[40:43], v1, s[12:15], 0 offen offset:768 sc0 sc1 nt\n"
      "v_pk_mul_f32 v[108:109], v[6:7], v[108:109] op_sel_hi:[0,1]\n"
      "v_pk_mul_f32 v[110:111], v[6:7], v[110:111] op_sel_hi:[0,1]\n"
      "v_pk_mul_f32 v[112:113], v[6:7], v[112:113] op_sel_hi:[0,1]\n"
      "v_pk_mul_f32 v[114:115], v[6:7], v[114:115] op_sel_hi:[0,1]\n"
      "v_cvt_pk_bf16_f32 v44, v108, v109\n"
      "v_cvt_pk_bf16_f32 v45, v110, v111\n"
      "v_cvt_pk_bf16_f32 v46, v112, v113\n"
      "v_cvt_pk_bf16_f32 v47, v114, v115\n"
      "buffer_store_dwordx4 v[44:47], v1, s[12:15], 0 offen offset:896 sc0 sc1 nt\n"
      "v_pk_mul_f32 v[116:117], v[6:7], v[116:117] op_sel_hi:[0,1]\n"
      "v_pk_mul_f32 v[118:119], v[6:7], v[118:119] op_sel_hi:[0,1]\n"
      "v_pk_mul_f32 v[120:121], v[6:7], v[120:121] op_sel_hi:[0,1]\n"
      "v_pk_mul_f32 v[122:123], v[6:7], v[122:123] op_sel_hi:[0,1]\n"
      "v_cvt_pk_bf16_f32 v48, v116, v117\n"
      "v_cvt_pk_bf16_f32 v49, v118, v119\n"
      "v_cvt_pk_bf16_f32 v50, v120, v121\n"
      "v_cvt_pk_bf16_f32 v51, v122, v123\n"
      "buffer_store_dwordx4 v[48:51], v1, s[12:15], 0 offen offset:1024 sc0 sc1 nt\n"
      :
      : "s"(__builtin_amdgcn_kernarg_segment_ptr()), "s"(__builtin_amdgcn_workgroup_id_x()),
        "v"(__builtin_amdgcn_workitem_id_x()), "s"(lds_base)
      :
        "memory", "scc", "vcc", "s4", "s5", "s6", "s7", "s8", "s9", "s10", "s11", "s12", "s13", "s14", "s15",
        "s16", "s17", "s18", "s19", "s20", "s21", "v1", "v2", "v3", "v4", "v5", "v6", "v7", "v8", "v9",
        "v10", "v11", "v12", "v13", "v14", "v15", "v16", "v17", "v18", "v19", "v20", "v21", "v22", "v23",
        "v24", "v25", "v26", "v27", "v28", "v29", "v30", "v31", "v32", "v33", "v34", "v35", "v36", "v37",
        "v38", "v39", "v40", "v41", "v42", "v43", "v44", "v45", "v46", "v47", "v48", "v49", "v50", "v51",
        "v52", "v53", "v54", "v55", "v56", "v57", "v58", "v59", "v60", "v61", "v62", "v63", "v64", "v65",
        "v66", "v67", "v68", "v69", "v70", "v71", "v72", "v73", "v74", "v75", "v76", "v77", "v78", "v79",
        "v80", "v81", "v82", "v83", "v84", "v85", "v86", "v87", "v88", "v89", "v90", "v91", "v92", "v93",
        "v94", "v95", "v96", "v97", "v98", "v99", "v100", "v101", "v102", "v103", "v104", "v105", "v106",
        "v107", "v108", "v109", "v110", "v111", "v112", "v113", "v114", "v115", "v116", "v117", "v118",
        "v119", "v120", "v121", "v122", "v123");
}

extern "C" void kernel_launch(void* const* d_in, const int* in_sizes, int n_in, void* d_out,
                              int out_size, void* d_ws, size_t ws_size, hipStream_t stream) {
  const int rows = in_sizes[0] / 576;
  if (rows <= 0) return;
  const unsigned bytes = (unsigned)rows * 1152u;
  const unsigned fmt = 0x00020000u;
  rmsnorm_kernel<<<(rows + 31) / 32, 256, 0, stream>>>(d_in[0], bytes, fmt, d_in[1], 1152u, fmt, d_out, bytes, fmt,
                                                       0u, 0u, 0u, 0u);
}
```
